```python
import torch

def _softmax(x, axis=-1):
    return torch.softmax(x, dim=axis)
def _gelu(x):
    return torch.nn.functional.gelu(x, approximate="tanh")
def _silu(x):
    return torch.nn.functional.silu(x)
def _sigmoid(x):
    return torch.sigmoid(x)
def _layernorm(x, axis=-1, eps=1e-5):
    m = x.mean(dim=axis, keepdim=True)
    v = x.var(dim=axis, keepdim=True, unbiased=False)
    return (x - m) / torch.sqrt(v + eps)
def _scatter_add(tgt, idx, src, axis=0):
    return tgt.clone().index_add_(axis, idx, src)
def _segment_sum(src, seg, n):
    out = torch.zeros((n,) + src.shape[1:], dtype=src.dtype)
    return out.index_add_(0, seg, src)
def _pad(x, pw):
    flat = tuple(v for pair in reversed(pw) for v in pair)
    return torch.nn.functional.pad(x, flat)

kernel_name = 'interleave_layers__linear_kernel_hedgehog__dense_activation_squared_relu__prompt'

def setup_inputs():

    _w = (lambda a: (lambda t: t.to(torch.bfloat16) if t.is_floating_point() else t)(torch.as_tensor(a)))
    def _A_setup():
        B, S, D, H = 1, 2048, 448, 16
        Dh = D // H
        return {
            "x": _w((lambda *s: torch.randn(*s, dtype=torch.bfloat16) * 0.05)(B, S, D)),
            "wqkv": _w((lambda *s: torch.randn(*s, dtype=torch.bfloat16) * 0.2)(D, 3 * D)),
            "w_phi": _w((lambda *s: torch.randn(*s, dtype=torch.bfloat16) * 0.2)(H, Dh, Dh) * (Dh ** -0.5)),
            "wo": _w((lambda *s: torch.randn(*s, dtype=torch.bfloat16) * 0.05)(D, D)),
        }
    _A_in = _A_setup()

    def _B_setup():
        B, S, D, F = 1, 2048, 448, 1792
        return {
            "x": _w((lambda *s: torch.randn(*s, dtype=torch.bfloat16) * 0.05)(B, S, D)),
            "w1": _w((lambda *s: torch.randn(*s, dtype=torch.bfloat16) * 0.05)(D, F)),
            "b1": _w((lambda *s: torch.randn(*s, dtype=torch.bfloat16) * 0.2)(F)),
            "w2": _w((lambda *s: torch.randn(*s, dtype=torch.bfloat16) * 0.05)(F, D)),
            "b2": _w((lambda *s: torch.randn(*s, dtype=torch.bfloat16) * 0.4)(D)),
        }
    _B_in = _B_setup()

    return {
        "A_x": _A_in["x"],
        "A_wqkv": _A_in["wqkv"],
        "A_w_phi": _A_in["w_phi"],
        "A_wo": _A_in["wo"],
        "B_x": _B_in["x"],
        "B_w1": _B_in["w1"],
        "B_b1": _B_in["b1"],
        "B_w2": _B_in["w2"],
        "B_b2": _B_in["b2"],
    }

def reference(A_x, A_wqkv, A_w_phi, A_wo, B_x, B_w1, B_b1, B_w2, B_b2):
    def _A_ref(x, wqkv, w_phi, wo):
        B, S, D = x.shape
        H = 16; Dh = D // H
        qkv = (lambda x, *d: x.permute(*d))((x @ wqkv).reshape(B, S, 3, H, Dh), 2, 0, 3, 1, 4)
        q, k, v = qkv[0], qkv[1], qkv[2]
        pq = torch.einsum("bhsd,hdf->bhsf", q, w_phi)
        pk = torch.einsum("bhsd,hdf->bhsf", k, w_phi)
        phi_q = torch.concatenate([_softmax(pq, axis=-1), _softmax(-pq, axis=-1)], axis=-1)
        phi_k = torch.concatenate([_softmax(pk, axis=-1), _softmax(-pk, axis=-1)], axis=-1)
        kv = torch.einsum("bhsf,bhsd->bhfd", phi_k, v)
        z  = torch.einsum("bhsf->bhf", phi_k)
        num = torch.einsum("bhqf,bhfd->bhqd", phi_q, kv)
        den = torch.einsum("bhqf,bhf->bhq", phi_q, z)[..., None] + 1e-6
        o = num / den
        return (lambda x, *d: x.permute(*d))(o, 0, 2, 1, 3).reshape(B, S, D) @ wo

    def _B_ref(x, w1, b1, w2, b2):
        h = torch.einsum("bsd,df->bsf", x, w1) + b1
        zero = torch.zeros_like(h)
        r = torch.where(h > zero, h, zero)
        h = r * r
        out = torch.einsum("bsf,fd->bsd", h, w2) + b2
        return out

    x = A_x
    x = x + _A_ref(_layernorm(x, axis=-1), A_wqkv, A_w_phi, A_wo)
    x = x + _B_ref(_layernorm(x, axis=-1), B_w1, B_b1, B_w2, B_b2)
    x = x + _A_ref(_layernorm(x, axis=-1), A_wqkv, A_w_phi, A_wo)
    x = x + _B_ref(_layernorm(x, axis=-1), B_w1, B_b1, B_w2, B_b2)
    return x
```

```cpp
#include <hip/hip_runtime.h>
#include <hip/hip_bf16.h>
#include <stdint.h>

#define S_TOK 2048
#define D_MODEL 448
#define N_HEAD 16
#define D_HEAD 28
#define D_FF 1792

using bf16x8 = __attribute__((ext_vector_type(8))) short;
using s16x4  = __attribute__((ext_vector_type(4))) short;
using f32x16 = __attribute__((ext_vector_type(16))) float;
using u32x4  = __attribute__((ext_vector_type(4))) unsigned;
using u32x2  = __attribute__((ext_vector_type(2))) unsigned;
typedef unsigned short u16;
typedef unsigned long long u64;

#define DI __device__ __forceinline__
#define MFMA32(a, b, c) __builtin_amdgcn_mfma_f32_32x32x16_bf16((a), (b), (c), 0, 0, 0)

DI float bf2f(u16 b) { return __uint_as_float(((unsigned)b) << 16); }
DI u16 f2bf(float f) {
  unsigned u = __float_as_uint(f);
  u += 0x7fffu + ((u >> 16) & 1u);
  return (u16)(u >> 16);
}
DI unsigned packbf(float lo, float hi) { return (unsigned)f2bf(lo) | ((unsigned)f2bf(hi) << 16); }
DI int crow(int reg, int h) { return (reg & 3) + 8 * (reg >> 2) + 4 * h; }
DI int krow(int s, int h, int j) { return 16 * s + 8 * (j >> 2) + 4 * h + (j & 3); }

DI s16x4 trd(const char* p) {
  return __builtin_amdgcn_ds_read_tr16_b64_v4i16(
      (__attribute__((address_space(3))) s16x4*)(uintptr_t)p);
}
DI bf16x8 cat4(s16x4 lo, s16x4 hi) { return __builtin_shufflevector(lo, hi, 0, 1, 2, 3, 4, 5, 6, 7); }

DI bf16x8 pack8(const float* v) {
  u32x4 u;
  u[0] = packbf(v[0], v[1]); u[1] = packbf(v[2], v[3]); u[2] = packbf(v[4], v[5]); u[3] = packbf(v[6], v[7]);
  return __builtin_bit_cast(bf16x8, u);
}

__global__ __launch_bounds__(256) void kA1(const u16* __restrict__ x, const u16* __restrict__ wqkv,
                                          const u16* __restrict__ wphi, u16* __restrict__ phiq,
                                          float* __restrict__ kvpart) {
  __shared__ __attribute__((aligned(16))) char smem[86016];
  const int tile = blockIdx.x >> 4, head = blockIdx.x & 15;
  const int tid = threadIdx.x, w = tid >> 6, lane = tid & 63, r = lane & 31, h = lane >> 5;
  const int i16 = lane & 15, q = i16 >> 2, p = i16 & 3, blk = (lane >> 4) & 1;

  for (int i = tid; i < 448 * 21; i += 256) {
    int row = i / 21, rem = i - row * 21, seg = rem / 7, c = rem - seg * 7;
    const char* src = (const char*)wqkv + (size_t)row * (3 * D_MODEL * 2) + (seg * D_MODEL + head * D_HEAD) * 2 + c * 8;
    *(u64*)(smem + row * 192 + seg * 64 + c * 8) = *(const u64*)src;
  }
  for (int i = tid; i < 448 * 3; i += 256) {
    int row = i / 3, seg = i - row * 3;
    *(u64*)(smem + row * 192 + seg * 64 + 56) = 0ull;
  }

  const int tok = tile * 128 + w * 32 + r;
  const char* xrow = (const char*)x + (size_t)tok * (D_MODEL * 2) + h * 16;
  bf16x8 xf[28];
#pragma unroll
  for (int s = 0; s < 28; ++s) xf[s] = *(const bf16x8*)(xrow + s * 32);
  float sum = 0.f;
#pragma unroll
  for (int s = 0; s < 28; ++s)
#pragma unroll
    for (int j = 0; j < 8; ++j) sum += bf2f((u16)xf[s][j]);
  sum += __shfl_xor(sum, 32);
  const float mean = sum * (1.f / D_MODEL);
  float var = 0.f;
#pragma unroll
  for (int s = 0; s < 28; ++s)
#pragma unroll
    for (int j = 0; j < 8; ++j) { float d = bf2f((u16)xf[s][j]) - mean; var += d * d; }
  var += __shfl_xor(var, 32);
  const float rstd = rsqrtf(var * (1.f / D_MODEL) + 1e-5f);
#pragma unroll
  for (int s = 0; s < 28; ++s) {
    float v8[8];
#pragma unroll
    for (int j = 0; j < 8; ++j) v8[j] = (bf2f((u16)xf[s][j]) - mean) * rstd;
    xf[s] = pack8(v8);
  }

  __syncthreads();

  f32x16 acc[3];
#pragma unroll
  for (int n = 0; n < 3; ++n)
#pragma unroll
    for (int i = 0; i < 16; ++i) acc[n][i] = 0.f;
#pragma unroll
  for (int s = 0; s < 28; ++s) {
    const char* wb = smem + (16 * s + 8 * h + q) * 192 + blk * 32 + p * 8;
#pragma unroll
    for (int n = 0; n < 3; ++n) {
      s16x4 lo = trd(wb + n * 64), hi = trd(wb + n * 64 + 4 * 192);
      acc[n] = MFMA32(cat4(lo, hi), xf[s], acc[n]);
    }
  }

  bf16x8 wpA[2];
#pragma unroll
  for (int s = 0; s < 2; ++s)
#pragma unroll
    for (int j = 0; j < 8; ++j) {
      int d = krow(s, h, j);
      wpA[s][j] = (d < D_HEAD && r < D_HEAD) ? (short)wphi[head * (D_HEAD * D_HEAD) + d * D_HEAD + r] : (short)0;
    }

  auto featmap = [&](const f32x16& X, float* pp, float* pm) {
    float t[16];
#pragma unroll
    for (int i = 0; i < 16; ++i) t[i] = bf2f(f2bf(X[i]));
    bf16x8 xb0 = pack8(t), xb1 = pack8(t + 8);
    f32x16 pacc;
#pragma unroll
    for (int i = 0; i < 16; ++i) pacc[i] = 0.f;
    pacc = MFMA32(wpA[0], xb0, pacc);
    pacc = MFMA32(wpA[1], xb1, pacc);
    float pv[16];
#pragma unroll
    for (int i = 0; i < 16; ++i) pv[i] = bf2f(f2bf(pacc[i]));
    float mx = -INFINITY, mn = INFINITY;
#pragma unroll
    for (int i = 0; i < 16; ++i)
      if (crow(i, h) < D_HEAD) { mx = fmaxf(mx, pv[i]); mn = fminf(mn, pv[i]); }
    mx = fmaxf(mx, __shfl_xor(mx, 32));
    mn = fminf(mn, __shfl_xor(mn, 32));
    float sp = 0.f, sm = 0.f;
#pragma unroll
    for (int i = 0; i < 16; ++i) {
      bool v = crow(i, h) < D_HEAD;
      pp[i] = v ? __expf(pv[i] - mx) : 0.f;
      pm[i] = v ? __expf(mn - pv[i]) : 0.f;
      sp += pp[i]; sm += pm[i];
    }
    sp += __shfl_xor(sp, 32);
    sm += __shfl_xor(sm, 32);
    float ip = 1.f / sp, im = 1.f / sm;
#pragma unroll
    for (int i = 0; i < 16; ++i) { pp[i] = bf2f(f2bf(pp[i] * ip)); pm[i] = bf2f(f2bf(pm[i] * im)); }
  };

  float qp[16], qm[16], kp[16], km[16];
  featmap(acc[0], qp, qm);
  featmap(acc[1], kp, km);

  {
    char* dst = (char*)phiq + ((size_t)tok * N_HEAD + head) * (56 * 2);
#pragma unroll
    for (int rg = 0; rg < 4; ++rg) {
      int f0 = 8 * rg + 4 * h;
      if (f0 + 3 < D_HEAD) {
        u32x2 a, b;
        a[0] = packbf(qp[4 * rg], qp[4 * rg + 1]); a[1] = packbf(qp[4 * rg + 2], qp[4 * rg + 3]);
        b[0] = packbf(qm[4 * rg], qm[4 * rg + 1]); b[1] = packbf(qm[4 * rg + 2], qm[4 * rg + 3]);
      *(u32x2*)(dst + f0 * 2) = a;
      *(u32x2*)(dst + 56 + f0 * 2) = b;
      }
    }
  }

  __syncthreads();
  char* phiK = smem;
  char* Vimg = smem + 64 * 272;
  if (w == 0) {
#pragma unroll
    for (int rr = 56; rr < 64; ++rr) *(u64*)(phiK + rr * 272 + r * 8) = 0ull;
#pragma unroll
    for (int rr = 29; rr < 32; ++rr) *(u64*)(Vimg + rr * 272 + r * 8) = 0ull;
  }
  {
    const int tl = w * 32 + r;
#pragma unroll
    for (int i = 0; i < 16; ++i) {
      int f = crow(i, h);
      if (f < D_HEAD) {
        *(u16*)(phiK + f * 272 + tl * 2) = f2bf(kp[i]);
        *(u16*)(phiK + (28 + f) * 272 + tl * 2) = f2bf(km[i]);
        *(u16*)(Vimg + f * 272 + tl * 2) = f2bf(acc[2][i]);
      }
    }
    if (h == 0) *(u16*)(Vimg + 28 * 272 + tl * 2) = f2bf(1.0f);
  }
  __syncthreads();
  if (w < 2) {
    f32x16 kacc;
#pragma unroll
    for (int i = 0; i < 16; ++i) kacc[i] = 0.f;
#pragma unroll
    for (int ks = 0; ks < 8; ++ks) {
      bf16x8 a = *(const bf16x8*)(phiK + (w * 32 + r) * 272 + (16 * ks + 8 * h) * 2);
      bf16x8 b = *(const bf16x8*)(Vimg + r * 272 + (16 * ks + 8 * h) * 2);
      kacc = MFMA32(a, b, kacc);
    }
    float* dst = kvpart + ((size_t)(tile * N_HEAD + head) * 64) * 32;
#pragma unroll
    for (int i = 0; i < 16; ++i) dst[(w * 32 + crow(i, h)) * 32 + r] = kacc[i];
  }
}

__global__ __launch_bounds__(256) void kR(const float* __restrict__ kvpart, u16* __restrict__ kvb) {
  int idx = blockIdx.x * 256 + threadIdx.x;
  int head = idx >> 11, f = (idx >> 5) & 63, d = idx & 31;
  float s = 0.f;
#pragma unroll
  for (int t = 0; t < 16; ++t) s += kvpart[((size_t)(t * N_HEAD + head) * 64 + f) * 32 + d];
  kvb[(head * 32 + d) * 64 + f] = f2bf(s);
}

__global__ __launch_bounds__(256) void kA2(const u16* __restrict__ phiq, const u16* __restrict__ kvb,
                                          const u16* __restrict__ wo, const u16* __restrict__ x,
                                          u16* __restrict__ x1) {
  __shared__ __attribute__((aligned(16))) char smem[58368 + 86016];
  char* otile = smem;
  char* wtile = smem + 58368;
  const int tt = blockIdx.x % 32, nc = blockIdx.x / 32;
  const int n0 = nc * 64;
  const int tid = threadIdx.x, w = tid >> 6, lane = tid & 63, r = lane & 31, h = lane >> 5;
  const int i16 = lane & 15, q = i16 >> 2, p = i16 & 3, blk = (lane >> 4) & 1;

  for (int i = tid; i < 448 * 8; i += 256) {
    int row = i >> 3, c = i & 7;
    *(u32x4*)(wtile + row * 192 + c * 16) =
        *(const u32x4*)((const char*)wo + (size_t)row * (D_MODEL * 2) + n0 * 2 + c * 16);
  }

  {
    const int tg = w & 1, hb = (w >> 1) * 8;
    const int tok = tt * 64 + tg * 32 + r;
    for (int hh = 0; hh < 8; ++hh) {
      const int head = hb + hh;
      const char* prow = (const char*)phiq + ((size_t)tok * N_HEAD + head) * 112 + h * 16;
      const char* krowp = (const char*)kvb + ((size_t)head * 32 + r) * 128 + h * 16;
      f32x16 acc;
#pragma unroll
      for (int i = 0; i < 16; ++i) acc[i] = 0.f;
#pragma unroll
      for (int s = 0; s < 4; ++s) {
        bf16x8 pf = (s == 3 && h == 1) ? bf16x8{0, 0, 0, 0, 0, 0, 0, 0} : *(const bf16x8*)(prow + s * 32);
        bf16x8 kf = *(const bf16x8*)(krowp + s * 32);
        acc = MFMA32(kf, pf, acc);
      }
      float den = __shfl(acc[12], (lane & 31) + 32) + 1e-6f;
      float inv = 1.f / den;
      char* dst = otile + (tg * 32 + r) * 912 + head * 56;
#pragma unroll
      for (int rg = 0; rg < 4; ++rg) {
        int d0 = 8 * rg + 4 * h;
        if (d0 + 3 < D_HEAD) {
          u32x2 o2;
          o2[0] = packbf(acc[4 * rg] * inv, acc[4 * rg + 1] * inv);
          o2[1] = packbf(acc[4 * rg + 2] * inv, acc[4 * rg + 3] * inv);
          *(u32x2*)(dst + d0 * 2) = o2;
        }
      }
    }
  }
  __syncthreads();

  const int tr_ = w & 1, tc = w >> 1;
  f32x16 acc;
#pragma unroll
  for (int i = 0; i < 16; ++i) acc[i] = 0.f;
#pragma unroll 4
  for (int s = 0; s < 28; ++s) {
    bf16x8 a = *(const bf16x8*)(otile + (tr_ * 32 + r) * 912 + (16 * s + 8 * h) * 2);
    const char* wb = wtile + (16 * s + 8 * h + q) * 192 + tc * 64 + blk * 32 + p * 8;
    s16x4 lo = trd(wb), hi = trd(wb + 4 * 192);
    acc = MFMA32(a, cat4(lo, hi), acc);
  }
  const int col = n0 + tc * 32 + r;
#pragma unroll
  for (int i = 0; i < 16; ++i) {
    int tok = tt * 64 + tr_ * 32 + crow(i, h);
    float xv = bf2f(x[(size_t)tok * D_MODEL + col]);
    x1[(size_t)tok * D_MODEL + col] = f2bf(xv + acc[i]);
  }
}

__global__ __launch_bounds__(256) void kB1(const u16* __restrict__ x1, const u16* __restrict__ w1,
                                          const u16* __restrict__ b1, u16* __restrict__ hout) {
  __shared__ __attribute__((aligned(16))) char smem[448 * 320];
  const int tile = blockIdx.x % 16, nc = blockIdx.x / 16;
  const int n0 = nc * 128;
  const int tid = threadIdx.x, w = tid >> 6, lane = tid & 63, r = lane & 31, h = lane >> 5;
  const int i16 = lane & 15, q = i16 >> 2, p = i16 & 3, blk = (lane >> 4) & 1;

  for (int i = tid; i < 448 * 16; i += 256) {
    int row = i >> 4, c = i & 15;
    *(u32x4*)(smem + row * 320 + c * 16) =
        *(const u32x4*)((const char*)w1 + (size_t)row * (D_FF * 2) + n0 * 2 + c * 16);
  }

  const int tok = tile * 128 + w * 32 + r;
  const char* xrow = (const char*)x1 + (size_t)tok * (D_MODEL * 2) + h * 16;
  bf16x8 xf[28];
#pragma unroll
  for (int s = 0; s < 28; ++s) xf[s] = *(const bf16x8*)(xrow + s * 32);
  float sum = 0.f;
#pragma unroll
  for (int s = 0; s < 28; ++s)
#pragma unroll
    for (int j = 0; j < 8; ++j) sum += bf2f((u16)xf[s][j]);
  sum += __shfl_xor(sum, 32);
  const float mean = sum * (1.f / D_MODEL);
  float var = 0.f;
#pragma unroll
  for (int s = 0; s < 28; ++s)
#pragma unroll
    for (int j = 0; j < 8; ++j) { float d = bf2f((u16)xf[s][j]) - mean; var += d * d; }
  var += __shfl_xor(var, 32);
  const float rstd = rsqrtf(var * (1.f / D_MODEL) + 1e-5f);
#pragma unroll
  for (int s = 0; s < 28; ++s) {
    float v8[8];
#pragma unroll
    for (int j = 0; j < 8; ++j) v8[j] = (bf2f((u16)xf[s][j]) - mean) * rstd;
    xf[s] = pack8(v8);
  }
  __syncthreads();

  f32x16 acc[4];
#pragma unroll
  for (int n = 0; n < 4; ++n)
#pragma unroll
    for (int i = 0; i < 16; ++i) acc[n][i] = 0.f;
#pragma unroll
  for (int s = 0; s < 28; ++s) {
    const char* wb = smem + (16 * s + 8 * h + q) * 320 + blk * 32 + p * 8;
#pragma unroll
    for (int n = 0; n < 4; ++n) {
      s16x4 lo = trd(wb + n * 64), hi = trd(wb + n * 64 + 4 * 320);
      acc[n] = MFMA32(cat4(lo, hi), xf[s], acc[n]);
    }
  }
  char* hrow = (char*)hout + (size_t)tok * (D_FF * 2);
#pragma unroll
  for (int n = 0; n < 4; ++n) {
#pragma unroll
    for (int rg = 0; rg < 4; ++rg) {
      int nn = n0 + n * 32 + 8 * rg + 4 * h;
      float v[4];
#pragma unroll
      for (int j = 0; j < 4; ++j) {
        float t = acc[n][4 * rg + j] + bf2f(b1[nn + j]);
        t = bf2f(f2bf(t));
        t = t > 0.f ? t : 0.f;
        v[j] = t * t;
      }
      u32x2 o2; o2[0] = packbf(v[0], v[1]); o2[1] = packbf(v[2], v[3]);
      *(u32x2*)(hrow + nn * 2) = o2;
    }
  }
}

#define B2_PF 7
__global__ __launch_bounds__(256) void kB2(const u16* __restrict__ hin, const u16* __restrict__ w2,
                                          const u16* __restrict__ b2, const u16* __restrict__ x1,
                                          u16* __restrict__ x2) {
  __shared__ __attribute__((aligned(16))) char smem[2 * (9216 + 12288)];
  const int tt = blockIdx.x % 32, nc = blockIdx.x / 32;
  const int n0 = nc * 64;
  const int tid = threadIdx.x, w = tid >> 6, lane = tid & 63, r = lane & 31, h = lane >> 5;
  const int i16 = lane & 15, q = i16 >> 2, p = i16 & 3, blk = (lane >> 4) & 1;
  const int tr_ = w & 1, tc = w >> 1;

  const int arow = tid >> 3, ac = tid & 7;
  const char* aptr = (const char*)hin + (size_t)(tt * 64 + arow) * (D_FF * 2) + ac * 16;
  const char* aptr2 = aptr + (size_t)32 * (D_FF * 2);
  const char* wptr = (const char*)w2 + (size_t)arow * (D_MODEL * 2) + n0 * 2 + ac * 16;
  const char* wptr2 = wptr + (size_t)32 * (D_MODEL * 2);

  u32x4 ra[B2_PF][2], rw[B2_PF][2];
#pragma unroll
  for (int i = 0; i < B2_PF; ++i) {
    ra[i][0] = *(const u32x4*)(aptr + i * 128);  ra[i][1] = *(const u32x4*)(aptr2 + i * 128);
    rw[i][0] = *(const u32x4*)(wptr + (size_t)i * 64 * (D_MODEL * 2));
    rw[i][1] = *(const u32x4*)(wptr2 + (size_t)i * 64 * (D_MODEL * 2));
  }

  f32x16 acc;
#pragma unroll
  for (int i = 0; i < 16; ++i) acc[i] = 0.f;

  for (int kt = 0; kt < 28; kt += B2_PF) {
#pragma unroll
    for (int i = 0; i < B2_PF; ++i) {
      const int step = kt + i;
      char* ab = smem + (step & 1) * (9216 + 12288);
      char* wb = ab + 9216;
      *(u32x4*)(ab + arow * 144 + ac * 16) = ra[i][0];
      *(u32x4*)(ab + (arow + 32) * 144 + ac * 16) = ra[i][1];
      *(u32x4*)(wb + arow * 192 + ac * 16) = rw[i][0];
      *(u32x4*)(wb + (arow + 32) * 192 + ac * 16) = rw[i][1];
      const int nstep = step + B2_PF;
      if (nstep < 28) {
        ra[i][0] = *(const u32x4*)(aptr + nstep * 128); ra[i][1] = *(const u32x4*)(aptr2 + nstep * 128);
        rw[i][0] = *(const u32x4*)(wptr + (size_t)nstep * 64 * (D_MODEL * 2));
        rw[i][1] = *(const u32x4*)(wptr2 + (size_t)nstep * 64 * (D_MODEL * 2));
      }
      __syncthreads();
#pragma unroll
      for (int s = 0; s < 4; ++s) {
        bf16x8 a = *(const bf16x8*)(ab + (tr_ * 32 + r) * 144 + (16 * s + 8 * h) * 2);
        const char* wp = wb + (16 * s + 8 * h + q) * 192 + tc * 64 + blk * 32 + p * 8;
        s16x4 lo = trd(wp), hi = trd(wp + 4 * 192);
        acc = MFMA32(a, cat4(lo, hi), acc);
      }
    }
  }
  const int col = n0 + tc * 32 + r;
  const float bb = bf2f(b2[col]);
#pragma unroll
  for (int i = 0; i < 16; ++i) {
    int tok = tt * 64 + tr_ * 32 + crow(i, h);
    float out = bf2f(f2bf(acc[i] + bb));
    float xv = bf2f(x1[(size_t)tok * D_MODEL + col]);
    x2[(size_t)tok * D_MODEL + col] = f2bf(xv + out);
  }
}

extern "C" void kernel_launch(void* const* d_in, const int* in_sizes, int n_in,
                              void* d_out, int out_size, void* d_ws, size_t ws_size,
                              hipStream_t stream) {
  const u16* x    = (const u16*)d_in[0];
  const u16* wqkv = (const u16*)d_in[1];
  const u16* wphi = (const u16*)d_in[2];
  const u16* wo   = (const u16*)d_in[3];
  const u16* w1   = (const u16*)d_in[5];
  const u16* b1   = (const u16*)d_in[6];
  const u16* w2   = (const u16*)d_in[7];
  const u16* b2   = (const u16*)d_in[8];
  u16* out = (u16*)d_out;

  char* ws = (char*)d_ws;
  size_t off = 0;
  u16* phiq = (u16*)(ws + off);   off += (size_t)S_TOK * N_HEAD * 56 * 2;
  float* kvpart = (float*)(ws + off); off += (size_t)16 * N_HEAD * 64 * 32 * 4;
  u16* kvb = (u16*)(ws + off);    off += (size_t)N_HEAD * 32 * 64 * 2;
  u16* x1 = (u16*)(ws + off);     off += (size_t)S_TOK * D_MODEL * 2;
  u16* hbuf = (u16*)(ws + off);   off += (size_t)S_TOK * D_FF * 2;
  u16* x2 = (u16*)(ws + off);     off += (size_t)S_TOK * D_MODEL * 2;

  for (int layer = 0; layer < 2; ++layer) {
    const u16* xin = layer == 0 ? x : x2;
    u16* xout = layer == 0 ? x2 : out;
    kA1<<<256, 256, 0, stream>>>(xin, wqkv, wphi, phiq, kvpart);
    kR<<<128, 256, 0, stream>>>(kvpart, kvb);
    kA2<<<224, 256, 0, stream>>>(phiq, kvb, wo, xin, x1);
    kB1<<<224, 256, 0, stream>>>(x1, w1, b1, hbuf);
    kB2<<<224, 256, 0, stream>>>(hbuf, w2, b2, x1, xout);
  }
}
```

```cpp
#include <hip/hip_runtime.h>
#include <hip/hip_bf16.h>
#include <stdint.h>

#define S_TOK 2048
#define D_MODEL 448
#define N_HEAD 16
#define D_HEAD 28
#define D_FF 1792

using bf16x8 = __attribute__((ext_vector_type(8))) short;
using s16x4  = __attribute__((ext_vector_type(4))) short;
using f32x16 = __attribute__((ext_vector_type(16))) float;
using u32x4  = __attribute__((ext_vector_type(4))) unsigned;
using u32x2  = __attribute__((ext_vector_type(2))) unsigned;
typedef unsigned short u16;
typedef unsigned long long u64;

#define DI __device__ __forceinline__
#define MFMA32(a, b, c) __builtin_amdgcn_mfma_f32_32x32x16_bf16((a), (b), (c), 0, 0, 0)

DI float bf2f(u16 b) { return __uint_as_float(((unsigned)b) << 16); }
DI u16 f2bf(float f) {
  unsigned u = __float_as_uint(f);
  u += 0x7fffu + ((u >> 16) & 1u);
  return (u16)(u >> 16);
}
DI unsigned packbf(float lo, float hi) { return (unsigned)f2bf(lo) | ((unsigned)f2bf(hi) << 16); }
DI int crow(int reg, int h) { return (reg & 3) + 8 * (reg >> 2) + 4 * h; }
DI int krow(int s, int h, int j) { return 16 * s + 8 * (j >> 2) + 4 * h + (j & 3); }

DI s16x4 trd(const char* p) {
  return __builtin_amdgcn_ds_read_tr16_b64_v4i16(
      (__attribute__((address_space(3))) s16x4*)(uintptr_t)p);
}
DI bf16x8 cat4(s16x4 lo, s16x4 hi) { return __builtin_shufflevector(lo, hi, 0, 1, 2, 3, 4, 5, 6, 7); }

DI void glds16(const void* g, char* lds_wave_base) {
  __builtin_amdgcn_global_load_lds((const __attribute__((address_space(1))) unsigned*)(uintptr_t)g,
                                   (__attribute__((address_space(3))) unsigned*)(uintptr_t)lds_wave_base, 16, 0, 0);
}
#define WAIT_VM(n) asm volatile("s_waitcnt vmcnt(" #n ")" ::: "memory")
#define RAW_BARRIER() do { asm volatile("s_waitcnt lgkmcnt(0)" ::: "memory"); __builtin_amdgcn_s_barrier(); } while (0)

DI bf16x8 pack8(const float* v) {
  u32x4 u;
  u[0] = packbf(v[0], v[1]); u[1] = packbf(v[2], v[3]); u[2] = packbf(v[4], v[5]); u[3] = packbf(v[6], v[7]);
  return __builtin_bit_cast(bf16x8, u);
}

__global__ __launch_bounds__(256) void kA1(const u16* __restrict__ x, const u16* __restrict__ wqkv,
                                          const u16* __restrict__ wphi, u16* __restrict__ phiq,
                                          float* __restrict__ kvpart) {
  __shared__ __attribute__((aligned(16))) char smem[86016 + 256];
  const int tile = blockIdx.x >> 4, head = blockIdx.x & 15;
  const int tid = threadIdx.x, w = tid >> 6, lane = tid & 63, r = lane & 31, h = lane >> 5;
  const int i16 = lane & 15, q = i16 >> 2, p = i16 & 3, blk = (lane >> 4) & 1;

  const int hoff = (head & 1) * 8;
#pragma unroll
  for (int it = 0; it < 21; ++it) {
    int c = it * 256 + tid;
    int row = c / 12, rem = c - row * 12, seg = rem >> 2, cc = rem & 3;
    const char* src = (const char*)wqkv + (size_t)row * (3 * D_MODEL * 2) + seg * (D_MODEL * 2) + ((head * 56) & ~15) + cc * 16;
    glds16(src, smem + (it * 256 + w * 64) * 16);
  }

  const int tok = tile * 128 + w * 32 + r;
  const char* xrow = (const char*)x + (size_t)tok * (D_MODEL * 2) + h * 16;
  bf16x8 xf[28];
#pragma unroll
  for (int s = 0; s < 28; ++s) xf[s] = *(const bf16x8*)(xrow + s * 32);
  float sum = 0.f;
#pragma unroll
  for (int s = 0; s < 28; ++s)
#pragma unroll
    for (int j = 0; j < 8; ++j) sum += bf2f((u16)xf[s][j]);
  sum += __shfl_xor(sum, 32);
  const float mean = sum * (1.f / D_MODEL);
  float var = 0.f;
#pragma unroll
  for (int s = 0; s < 28; ++s)
#pragma unroll
    for (int j = 0; j < 8; ++j) { float d = bf2f((u16)xf[s][j]) - mean; var += d * d; }
  var += __shfl_xor(var, 32);
  const float rstd = rsqrtf(var * (1.f / D_MODEL) + 1e-5f);
#pragma unroll
  for (int s = 0; s < 28; ++s) {
    float v8[8];
#pragma unroll
    for (int j = 0; j < 8; ++j) v8[j] = (bf2f((u16)xf[s][j]) - mean) * rstd;
    xf[s] = pack8(v8);
  }

  WAIT_VM(0);
  __syncthreads();

  f32x16 acc[3];
#pragma unroll
  for (int n = 0; n < 3; ++n)
#pragma unroll
    for (int i = 0; i < 16; ++i) acc[n][i] = 0.f;
#pragma unroll
  for (int s = 0; s < 28; ++s) {
    const char* wb = smem + (16 * s + 8 * h + q) * 192 + blk * 32 + p * 8 + hoff;
#pragma unroll
    for (int n = 0; n < 3; ++n) {
      s16x4 lo = trd(wb + n * 64), hi = trd(wb + n * 64 + 4 * 192);
      acc[n] = MFMA32(cat4(lo, hi), xf[s], acc[n]);
    }
  }

  bf16x8 wpA[2];
#pragma unroll
  for (int s = 0; s < 2; ++s)
#pragma unroll
    for (int j = 0; j < 8; ++j) {
      int d = krow(s, h, j);
      wpA[s][j] = (d < D_HEAD && r < D_HEAD) ? (short)wphi[head * (D_HEAD * D_HEAD) + d * D_HEAD + r] : (short)0;
    }

  auto featmap = [&](const f32x16& X, float* pp, float* pm) {
    float t[16];
#pragma unroll
    for (int i = 0; i < 16; ++i) t[i] = bf2f(f2bf(X[i]));
    bf16x8 xb0 = pack8(t), xb1 = pack8(t + 8);
    f32x16 pacc;
#pragma unroll
    for (int i = 0; i < 16; ++i) pacc[i] = 0.f;
    pacc = MFMA32(wpA[0], xb0, pacc);
    pacc = MFMA32(wpA[1], xb1, pacc);
    float pv[16];
#pragma unroll
    for (int i = 0; i < 16; ++i) pv[i] = bf2f(f2bf(pacc[i]));
    float mx = -INFINITY, mn = INFINITY;
#pragma unroll
    for (int i = 0; i < 16; ++i)
      if (crow(i, h) < D_HEAD) { mx = fmaxf(mx, pv[i]); mn = fminf(mn, pv[i]); }
    mx = fmaxf(mx, __shfl_xor(mx, 32));
    mn = fminf(mn, __shfl_xor(mn, 32));
    float sp = 0.f, sm = 0.f;
#pragma unroll
    for (int i = 0; i < 16; ++i) {
      bool v = crow(i, h) < D_HEAD;
      pp[i] = v ? __expf(pv[i] - mx) : 0.f;
      pm[i] = v ? __expf(mn - pv[i]) : 0.f;
      sp += pp[i]; sm += pm[i];
    }
    sp += __shfl_xor(sp, 32);
    sm += __shfl_xor(sm, 32);
    float ip = 1.f / sp, im = 1.f / sm;
#pragma unroll
    for (int i = 0; i < 16; ++i) { pp[i] = bf2f(f2bf(pp[i] * ip)); pm[i] = bf2f(f2bf(pm[i] * im)); }
  };

  float qp[16], qm[16], kp[16], km[16];
  featmap(acc[0], qp, qm);
  featmap(acc[1], kp, km);

  {
    char* dst = (char*)phiq + ((size_t)tok * N_HEAD + head) * (56 * 2);
#pragma unroll
    for (int rg = 0; rg < 4; ++rg) {
      int f0 = 8 * rg + 4 * h;
      if (f0 + 3 < D_HEAD) {
        u32x2 a, b;
        a[0] = packbf(qp[4 * rg], qp[4 * rg + 1]); a[1] = packbf(qp[4 * rg + 2], qp[4 * rg + 3]);
        b[0] = packbf(qm[4 * rg], qm[4 * rg + 1]); b[1] = packbf(qm[4 * rg + 2], qm[4 * rg + 3]);
      *(u32x2*)(dst + f0 * 2) = a;
      *(u32x2*)(dst + 56 + f0 * 2) = b;
      }
    }
  }

  __syncthreads();
  char* phiK = smem;
  char* Vimg = smem + 64 * 272;
  if (w == 0) {
#pragma unroll
    for (int rr = 56; rr < 64; ++rr) *(u64*)(phiK + rr * 272 + r * 8) = 0ull;
#pragma unroll
    for (int rr = 29; rr < 32; ++rr) *(u64*)(Vimg + rr * 272 + r * 8) = 0ull;
  }
  {
    const int tl = w * 32 + r;
#pragma unroll
    for (int i = 0; i < 16; ++i) {
      int f = crow(i, h);
      if (f < D_HEAD) {
        *(u16*)(phiK + f * 272 + tl * 2) = f2bf(kp[i]);
        *(u16*)(phiK + (28 + f) * 272 + tl * 2) = f2bf(km[i]);
        *(u16*)(Vimg + f * 272 + tl * 2) = f2bf(acc[2][i]);
      }
    }
    if (h == 0) *(u16*)(Vimg + 28 * 272 + tl * 2) = f2bf(1.0f);
  }
  __syncthreads();
  if (w < 2) {
    f32x16 kacc;
#pragma unroll
    for (int i = 0; i < 16; ++i) kacc[i] = 0.f;
#pragma unroll
    for (int ks = 0; ks < 8; ++ks) {
      bf16x8 a = *(const bf16x8*)(phiK + (w * 32 + r) * 272 + (16 * ks + 8 * h) * 2);
      bf16x8 b = *(const bf16x8*)(Vimg + r * 272 + (16 * ks + 8 * h) * 2);
      kacc = MFMA32(a, b, kacc);
    }
    float* dst = kvpart + ((size_t)(tile * N_HEAD + head) * 64) * 32;
#pragma unroll
    for (int i = 0; i < 16; ++i) dst[(w * 32 + crow(i, h)) * 32 + r] = kacc[i];
  }
}

__global__ __launch_bounds__(256) void kR(const float* __restrict__ kvpart, u16* __restrict__ kvb) {
  int idx = blockIdx.x * 256 + threadIdx.x;
  int head = idx >> 11, f = (idx >> 5) & 63, d = idx & 31;
  float s = 0.f;
#pragma unroll
  for (int t = 0; t < 16; ++t) s += kvpart[((size_t)(t * N_HEAD + head) * 64 + f) * 32 + d];
  kvb[(head * 32 + d) * 64 + f] = f2bf(s);
}

__global__ __launch_bounds__(256) void kA2(const u16* __restrict__ phiq, const u16* __restrict__ kvb,
                                          const u16* __restrict__ wo, const u16* __restrict__ x,
                                          u16* __restrict__ x1) {
  __shared__ __attribute__((aligned(16))) char smem[58368 + 57344];
  char* otile = smem;
  char* wtile = smem + 58368;
  const int tt = blockIdx.x % 32, nc = blockIdx.x / 32;
  const int n0 = nc * 64;
  const int tid = threadIdx.x, w = tid >> 6, lane = tid & 63, r = lane & 31, h = lane >> 5;
  const int i16 = lane & 15, q = i16 >> 2, p = i16 & 3, blk = (lane >> 4) & 1;

#pragma unroll
  for (int it = 0; it < 14; ++it) {
    int c = it * 256 + tid;
    int row = c >> 3, ch = (c & 7) ^ ((row & 2) << 1);
    glds16((const char*)wo + (size_t)row * (D_MODEL * 2) + n0 * 2 + ch * 16, wtile + (it * 256 + w * 64) * 16);
  }

  {
    const int tg = w & 1, hb = (w >> 1) * 8;
    const int tok = tt * 64 + tg * 32 + r;
#pragma unroll
    for (int hq = 0; hq < 2; ++hq) {
      bf16x8 pf[4][4], kf[4][4];
#pragma unroll
      for (int hh = 0; hh < 4; ++hh) {
        const int head = hb + hq * 4 + hh;
        const char* prow = (const char*)phiq + ((size_t)tok * N_HEAD + head) * 112 + h * 16;
        const char* krowp = (const char*)kvb + ((size_t)head * 32 + r) * 128 + h * 16;
#pragma unroll
        for (int s = 0; s < 4; ++s) {
          pf[hh][s] = *(const bf16x8*)(prow + ((s == 3) ? (96 - h * 16) : s * 32));
          kf[hh][s] = *(const bf16x8*)(krowp + s * 32);
        }
      }
#pragma unroll
      for (int hh = 0; hh < 4; ++hh) {
        const int head = hb + hq * 4 + hh;
        if (h == 1) pf[hh][3] = bf16x8{0, 0, 0, 0, 0, 0, 0, 0};
        f32x16 acc;
#pragma unroll
        for (int i = 0; i < 16; ++i) acc[i] = 0.f;
#pragma unroll
        for (int s = 0; s < 4; ++s) acc = MFMA32(kf[hh][s], pf[hh][s], acc);
        float den = __shfl(acc[12], (lane & 31) + 32) + 1e-6f;
        float inv = 1.f / den;
        char* dst = otile + (tg * 32 + r) * 912 + head * 56;
#pragma unroll
        for (int rg = 0; rg < 4; ++rg) {
          int d0 = 8 * rg + 4 * h;
          if (d0 + 3 < D_HEAD) {
            u32x2 o2;
            o2[0] = packbf(acc[4 * rg] * inv, acc[4 * rg + 1] * inv);
            o2[1] = packbf(acc[4 * rg + 2] * inv, acc[4 * rg + 3] * inv);
            *(u32x2*)(dst + d0 * 2) = o2;
          }
        }
      }
    }
  }
  WAIT_VM(0);
  __syncthreads();

  const int tr_ = w & 1, tc = w >> 1;
  f32x16 acc;
#pragma unroll
  for (int i = 0; i < 16; ++i) acc[i] = 0.f;
#pragma unroll 4
  for (int s = 0; s < 28; ++s) {
    bf16x8 a = *(const bf16x8*)(otile + (tr_ * 32 + r) * 912 + (16 * s + 8 * h) * 2);
    const char* wb = wtile + (16 * s + 8 * h + q) * 128 + (((tc * 4 + blk * 2 + (p >> 1)) ^ ((q & 2) << 1)) * 16) + (p & 1) * 8;
    s16x4 lo = trd(wb), hi = trd(wb + 4 * 128);
    acc = MFMA32(a, cat4(lo, hi), acc);
  }
  const int col = n0 + tc * 32 + r;
#pragma unroll
  for (int i = 0; i < 16; ++i) {
    int tok = tt * 64 + tr_ * 32 + crow(i, h);
    float xv = bf2f(x[(size_t)tok * D_MODEL + col]);
    x1[(size_t)tok * D_MODEL + col] = f2bf(xv + acc[i]);
  }
}

__global__ __launch_bounds__(256) void kB1(const u16* __restrict__ x1, const u16* __restrict__ w1,
                                          const u16* __restrict__ b1, u16* __restrict__ hout) {
  __shared__ __attribute__((aligned(16))) char smem[448 * 256];
  const int tile = blockIdx.x % 16, nc = blockIdx.x / 16;
  const int n0 = nc * 128;
  const int tid = threadIdx.x, w = tid >> 6, lane = tid & 63, r = lane & 31, h = lane >> 5;
  const int i16 = lane & 15, q = i16 >> 2, p = i16 & 3, blk = (lane >> 4) & 1;

#pragma unroll
  for (int it = 0; it < 28; ++it) {
    int c = it * 256 + tid;
    int row = c >> 4, ch = (c & 15) ^ ((row & 3) << 2);
    glds16((const char*)w1 + (size_t)row * (D_FF * 2) + n0 * 2 + ch * 16, smem + (it * 256 + w * 64) * 16);
  }

  const int tok = tile * 128 + w * 32 + r;
  const char* xrow = (const char*)x1 + (size_t)tok * (D_MODEL * 2) + h * 16;
  bf16x8 xf[28];
#pragma unroll
  for (int s = 0; s < 28; ++s) xf[s] = *(const bf16x8*)(xrow + s * 32);
  float sum = 0.f;
#pragma unroll
  for (int s = 0; s < 28; ++s)
#pragma unroll
    for (int j = 0; j < 8; ++j) sum += bf2f((u16)xf[s][j]);
  sum += __shfl_xor(sum, 32);
  const float mean = sum * (1.f / D_MODEL);
  float var = 0.f;
#pragma unroll
  for (int s = 0; s < 28; ++s)
#pragma unroll
    for (int j = 0; j < 8; ++j) { float d = bf2f((u16)xf[s][j]) - mean; var += d * d; }
  var += __shfl_xor(var, 32);
  const float rstd = rsqrtf(var * (1.f / D_MODEL) + 1e-5f);
#pragma unroll
  for (int s = 0; s < 28; ++s) {
    float v8[8];
#pragma unroll
    for (int j = 0; j < 8; ++j) v8[j] = (bf2f((u16)xf[s][j]) - mean) * rstd;
    xf[s] = pack8(v8);
  }
  WAIT_VM(0);
  __syncthreads();

  f32x16 acc[4];
#pragma unroll
  for (int n = 0; n < 4; ++n)
#pragma unroll
    for (int i = 0; i < 16; ++i) acc[n][i] = 0.f;
#pragma unroll
  for (int s = 0; s < 28; ++s) {
    const char* wb = smem + (16 * s + 8 * h + q) * 256 + (p & 1) * 8;
#pragma unroll
    for (int n = 0; n < 4; ++n) {
      const int off = ((n * 4 + blk * 2 + (p >> 1)) ^ (q << 2)) * 16;
      s16x4 lo = trd(wb + off), hi = trd(wb + off + 4 * 256);
      acc[n] = MFMA32(cat4(lo, hi), xf[s], acc[n]);
    }
  }
  char* hrow = (char*)hout + (size_t)tok * (D_FF * 2);
#pragma unroll
  for (int n = 0; n < 4; ++n) {
#pragma unroll
    for (int rg = 0; rg < 4; ++rg) {
      int nn = n0 + n * 32 + 8 * rg + 4 * h;
      float v[4];
#pragma unroll
      for (int j = 0; j < 4; ++j) {
        float t = acc[n][4 * rg + j] + bf2f(b1[nn + j]);
        t = bf2f(f2bf(t));
        t = t > 0.f ? t : 0.f;
        v[j] = t * t;
      }
      u32x2 o2; o2[0] = packbf(v[0], v[1]); o2[1] = packbf(v[2], v[3]);
      *(u32x2*)(hrow + nn * 2) = o2;
    }
  }
}

#define B2_NS 6
__global__ __launch_bounds__(256) void kB2(const u16* __restrict__ hin, const u16* __restrict__ w2,
                                          const u16* __restrict__ b2, const u16* __restrict__ x1,
                                          u16* __restrict__ x2) {
  __shared__ __attribute__((aligned(16))) char smem[B2_NS * 16384];
  const int tt = blockIdx.x % 32, nc = blockIdx.x / 32;
  const int n0 = nc * 64;
  const int tid = threadIdx.x, w = tid >> 6, lane = tid & 63, r = lane & 31, h = lane >> 5;
  const int i16 = lane & 15, q = i16 >> 2, p = i16 & 3, blk = (lane >> 4) & 1;
  const int tr_ = w & 1, tc = w >> 1;

  const int srow = tid >> 3, sc = tid & 7;
  const char* a0 = (const char*)hin + (size_t)(tt * 64 + srow) * (D_FF * 2) + ((sc ^ (srow & 7)) * 16);
  const char* a1 = (const char*)hin + (size_t)(tt * 64 + srow + 32) * (D_FF * 2) + ((sc ^ (srow & 7)) * 16);
  const char* w0 = (const char*)w2 + (size_t)srow * (D_MODEL * 2) + n0 * 2 + ((sc ^ ((srow & 2) << 1)) * 16);
  const char* w1p = w0 + (size_t)32 * (D_MODEL * 2);

  auto issue = [&](int st, int slot) {
    st = st > 27 ? 27 : st;
    char* base = smem + slot * 16384 + w * 1024;
    glds16(a0 + st * 128, base);
    glds16(a1 + st * 128, base + 4096);
    glds16(w0 + (size_t)st * 64 * (D_MODEL * 2), base + 8192);
    glds16(w1p + (size_t)st * 64 * (D_MODEL * 2), base + 12288);
  };
#pragma unroll
  for (int i = 0; i < B2_NS - 1; ++i) issue(i, i);

  f32x16 acc;
#pragma unroll
  for (int i = 0; i < 16; ++i) acc[i] = 0.f;

  int slot = 0, islot = B2_NS - 1;
  for (int step = 0; step < 28; ++step) {
    WAIT_VM(16);
    RAW_BARRIER();
    issue(step + B2_NS - 1, islot);
    const char* ab = smem + slot * 16384;
    const char* wb = ab + 8192;
    const int arow = tr_ * 32 + r;
#pragma unroll
    for (int s = 0; s < 4; ++s) {
      bf16x8 a = *(const bf16x8*)(ab + arow * 128 + (((2 * s + h) ^ (arow & 7)) * 16));
      const char* wp = wb + (16 * s + 8 * h + q) * 128 + (((tc * 4 + blk * 2 + (p >> 1)) ^ ((q & 2) << 1)) * 16) + (p & 1) * 8;
      s16x4 lo = trd(wp), hi = trd(wp + 4 * 128);
      acc = MFMA32(a, cat4(lo, hi), acc);
    }
    slot = slot + 1 == B2_NS ? 0 : slot + 1;
    islot = islot + 1 == B2_NS ? 0 : islot + 1;
  }
  WAIT_VM(0);
  const int col = n0 + tc * 32 + r;
  const float bb = bf2f(b2[col]);
#pragma unroll
  for (int i = 0; i < 16; ++i) {
    int tok = tt * 64 + tr_ * 32 + crow(i, h);
    float out = bf2f(f2bf(acc[i] + bb));
    float xv = bf2f(x1[(size_t)tok * D_MODEL + col]);
    x2[(size_t)tok * D_MODEL + col] = f2bf(xv + out);
  }
}

extern "C" void kernel_launch(void* const* d_in, const int* in_sizes, int n_in,
                              void* d_out, int out_size, void* d_ws, size_t ws_size,
                              hipStream_t stream) {
  const u16* x    = (const u16*)d_in[0];
  const u16* wqkv = (const u16*)d_in[1];
  const u16* wphi = (const u16*)d_in[2];
  const u16* wo   = (const u16*)d_in[3];
  const u16* w1   = (const u16*)d_in[5];
  const u16* b1   = (const u16*)d_in[6];
  const u16* w2   = (const u16*)d_in[7];
  const u16* b2   = (const u16*)d_in[8];
  u16* out = (u16*)d_out;

  char* ws = (char*)d_ws;
  size_t off = 0;
  u16* phiq = (u16*)(ws + off);   off += (size_t)S_TOK * N_HEAD * 56 * 2;
  float* kvpart = (float*)(ws + off); off += (size_t)16 * N_HEAD * 64 * 32 * 4;
  u16* kvb = (u16*)(ws + off);    off += (size_t)N_HEAD * 32 * 64 * 2;
  u16* x1 = (u16*)(ws + off);     off += (size_t)S_TOK * D_MODEL * 2;
  u16* hbuf = (u16*)(ws + off);   off += (size_t)S_TOK * D_FF * 2;
  u16* x2 = (u16*)(ws + off);     off += (size_t)S_TOK * D_MODEL * 2;

  for (int layer = 0; layer < 2; ++layer) {
    const u16* xin = layer == 0 ? x : x2;
    u16* xout = layer == 0 ? x2 : out;
    kA1<<<256, 256, 0, stream>>>(xin, wqkv, wphi, phiq, kvpart);
    kR<<<128, 256, 0, stream>>>(kvpart, kvb);
    kA2<<<224, 256, 0, stream>>>(phiq, kvb, wo, xin, x1);
    kB1<<<224, 256, 0, stream>>>(x1, w1, b1, hbuf);
    kB2<<<224, 256, 0, stream>>>(hbuf, w2, b2, x1, xout);
  }
}
```
